```python
import torch

def _softmax(x, axis=-1):
    return torch.softmax(x, dim=axis)
def _gelu(x):
    return torch.nn.functional.gelu(x, approximate="tanh")
def _silu(x):
    return torch.nn.functional.silu(x)
def _sigmoid(x):
    return torch.sigmoid(x)
def _layernorm(x, axis=-1, eps=1e-5):
    m = x.mean(dim=axis, keepdim=True)
    v = x.var(dim=axis, keepdim=True, unbiased=False)
    return (x - m) / torch.sqrt(v + eps)
def _scatter_add(tgt, idx, src, axis=0):
    return tgt.clone().index_add_(axis, idx, src)
def _segment_sum(src, seg, n):
    out = torch.zeros((n,) + src.shape[1:], dtype=src.dtype)
    return out.index_add_(0, seg, src)
def _pad(x, pw):
    flat = tuple(v for pair in reversed(pw) for v in pair)
    return torch.nn.functional.pad(x, flat)

kernel_name = 'standalone__kv_cache_quant_int4_int2_kv_with_per_channel_or_per_token_scales__sampling'

def setup_inputs():

    _w = (lambda a: (lambda t: t.to(torch.bfloat16) if t.is_floating_point() else t)(torch.as_tensor(a)))
    B = 8
    Sq = 4
    Skv = 2048
    Hq = 32
    Hkv = 16
    Dh = 128
    bits = 8
    G = 128
    if G <= 0 or G >= Skv:
        G = max(1, Skv // 2)
    Skv = (Skv // G) * G
    ngk = Skv // G
    qmax = (1 << bits) - 1
    return {
        "q": _w((lambda *s: torch.randn(*s, dtype=torch.bfloat16) * 0.4)(B, Hq, Sq, Dh)),
        "qk": _w(torch.randint(0, qmax + 1, (B, Hkv, Skv, Dh)).to(torch.int32)),
        "qv": _w(torch.randint(0, qmax + 1, (B, Hkv, Skv, Dh)).to(torch.int32)),
        "k_scales": _w(torch.abs((lambda *s: torch.randn(*s, dtype=torch.bfloat16) * 0.4)(B, Hkv, ngk, Dh)) * 0.05 + 0.02),
        "k_zeros": _w((lambda *s: torch.randn(*s, dtype=torch.bfloat16) * 0.4)(B, Hkv, ngk, Dh) + float(qmax) / 2.0),
        "v_scales": _w(torch.abs((lambda *s: torch.randn(*s, dtype=torch.bfloat16) * 0.05)(B, Hkv, Skv, 1)) * 0.05 + 0.02),
        "v_zeros": _w((lambda *s: torch.randn(*s, dtype=torch.bfloat16) * 0.05)(B, Hkv, Skv, 1) + float(qmax) / 2.0),
    }

def reference(q, qk, qv, k_scales, k_zeros, v_scales, v_zeros):
    B, Hq, Sq, Dh = q.shape
    Hkv, Skv = qk.shape[1], qk.shape[2]
    ngk = k_scales.shape[2]
    G = Skv // ngk
    ks = (lambda x, n, axis: torch.repeat_interleave(x, n, dim=axis))(k_scales, G, axis=2)
    kz = (lambda x, n, axis: torch.repeat_interleave(x, n, dim=axis))(k_zeros, G, axis=2)
    k = ((lambda x, dt: x.to(dt))(qk, q.dtype) - kz) * ks
    v = ((lambda x, dt: x.to(dt))(qv, q.dtype) - v_zeros) * v_scales
    rep = Hq // Hkv
    if rep > 1:
        k = (lambda x, n, axis: torch.repeat_interleave(x, n, dim=axis))(k, rep, axis=1)
        v = (lambda x, n, axis: torch.repeat_interleave(x, n, dim=axis))(v, rep, axis=1)
    scale = float(Dh) ** -0.5
    logits = torch.einsum("bhqd,bhkd->bhqk", q, k) * scale
    p = _softmax(logits, axis=-1)
    o = torch.einsum("bhqk,bhkd->bhqd", p, v)
    return o
```

```cpp
#include <hip/hip_runtime.h>
#include <hip/hip_bf16.h>
#include <cstdint>

namespace {

constexpr int B = 8, HQ = 32, HKV = 16, SQ = 4, SKV = 2048, D = 128, GRP = 128, NGK = SKV / GRP;
constexpr int MROWS = (HQ / HKV) * SQ;
constexpr int NSPLIT = 2, WAVES = 8, NTHR = WAVES * 64;
constexpr int KPW = SKV / NSPLIT / WAVES;
constexpr int NIT = KPW / 32;
static_assert(KPW == GRP, "one K group per wave");

typedef unsigned u32x4 __attribute__((ext_vector_type(4)));
typedef unsigned u32x2 __attribute__((ext_vector_type(2)));
typedef int i32x4 __attribute__((ext_vector_type(4)));
typedef float f32x4 __attribute__((ext_vector_type(4)));
typedef float f32x2 __attribute__((ext_vector_type(2)));
typedef __bf16 bf16x2 __attribute__((ext_vector_type(2)));
typedef __bf16 bf16x8 __attribute__((ext_vector_type(8)));

__device__ __forceinline__ unsigned cvtpk(float lo, float hi) {
  f32x2 v = {lo, hi};
  return __builtin_bit_cast(unsigned, __builtin_convertvector(v, bf16x2));
}
__device__ __forceinline__ float bflo(unsigned u) { return __builtin_bit_cast(float, u << 16); }
__device__ __forceinline__ float bfhi(unsigned u) { return __builtin_bit_cast(float, u & 0xffff0000u); }
template <int CTRL> __device__ __forceinline__ float dpp(float x) {
  return __builtin_bit_cast(float, __builtin_amdgcn_mov_dpp(__builtin_bit_cast(int, x), CTRL, 0xf, 0xf, true));
}
constexpr int ROR8 = 0x128;
__device__ __forceinline__ float xg_max(float x) {
  auto s = __builtin_amdgcn_permlane16_swap(__float_as_uint(x), __float_as_uint(x), false, false);
  x = fmaxf(__uint_as_float(s[0]), __uint_as_float(s[1]));
  auto t = __builtin_amdgcn_permlane32_swap(__float_as_uint(x), __float_as_uint(x), false, false);
  return fmaxf(__uint_as_float(t[0]), __uint_as_float(t[1]));
}
__device__ __forceinline__ float xg_sum(float x) {
  auto s = __builtin_amdgcn_permlane16_swap(__float_as_uint(x), __float_as_uint(x), false, false);
  x = __uint_as_float(s[0]) + __uint_as_float(s[1]);
  auto t = __builtin_amdgcn_permlane32_swap(__float_as_uint(x), __float_as_uint(x), false, false);
  return __uint_as_float(t[0]) + __uint_as_float(t[1]);
}
__device__ __forceinline__ f32x4 mfma16(u32x4 a, u32x4 b, f32x4 c) {
  return __builtin_amdgcn_mfma_f32_16x16x32_bf16(__builtin_bit_cast(bf16x8, a), __builtin_bit_cast(bf16x8, b), c, 0, 0, 0);
}

constexpr int KPITCH = 272;
constexpr int KT_BYTES = 32 * KPITCH;
constexpr int REC_FLOATS = 16 + MROWS * D;
constexpr int LDS_BYTES = WAVES * KT_BYTES;
static_assert(WAVES * REC_FLOATS * 4 <= LDS_BYTES, "");
static_assert(REC_FLOATS * 4 <= KT_BYTES, "");

constexpr int AUX_NT = 2;

__global__ void __launch_bounds__(NTHR, 2)
attn_split(const unsigned short* __restrict__ q, const int* __restrict__ qk, const int* __restrict__ qv,
           const unsigned short* __restrict__ ks, const unsigned short* __restrict__ kz,
           const unsigned short* __restrict__ vs, const unsigned short* __restrict__ vz,
           float* __restrict__ part) {
  __shared__ __attribute__((aligned(16))) unsigned char lds[LDS_BYTES];
  const int tid = threadIdx.x, lane = tid & 63, wave = __builtin_amdgcn_readfirstlane(tid >> 6);
  const int g = lane >> 4, c = lane & 15;
  const int bh = blockIdx.x >> 1, split = blockIdx.x & 1;
  const int key0 = split * (SKV / NSPLIT) + wave * KPW;
  const int grp = key0 / GRP;

  u32x4 qraw[4], ksr[4], kzr[4];
  {
    const unsigned short* qp = q + (size_t)bh * (MROWS * D) + (c & 7) * D + 8 * g;
    const unsigned short* sp = ks + ((size_t)bh * NGK + grp) * D + 8 * g;
    const unsigned short* zp = kz + ((size_t)bh * NGK + grp) * D + 8 * g;
#pragma unroll
    for (int s = 0; s < 4; ++s) {
      qraw[s] = *reinterpret_cast<const u32x4*>(qp + 32 * s);
      ksr[s] = *reinterpret_cast<const u32x4*>(sp + 32 * s);
      kzr[s] = *reinterpret_cast<const u32x4*>(zp + 32 * s);
    }
  }
  const size_t kvoff = ((size_t)bh * SKV + key0) * D;
  const __amdgpu_buffer_rsrc_t rk = __builtin_amdgcn_make_buffer_rsrc((void*)(qk + kvoff), 0, KPW * D * 4, 0x00020000);
  const __amdgpu_buffer_rsrc_t rv = __builtin_amdgcn_make_buffer_rsrc((void*)(qv + kvoff), 0, KPW * D * 4, 0x00020000);
  const __amdgpu_buffer_rsrc_t rs = __builtin_amdgcn_make_buffer_rsrc((void*)(vs + (size_t)bh * SKV + key0), 0, KPW * 2, 0x00020000);
  const __amdgpu_buffer_rsrc_t rz = __builtin_amdgcn_make_buffer_rsrc((void*)(vz + (size_t)bh * SKV + key0), 0, KPW * 2, 0x00020000);
  const int koff = lane * 16;
  const int voff = g * (8 * 512) + c * 16;
  const int soff = g * 16;

  u32x4 K[16], V[16], vsr, vzr;
#pragma unroll
  for (int n = 0; n < 16; ++n) K[n] = __builtin_amdgcn_raw_buffer_load_b128(rk, koff + n * 1024, 0, AUX_NT);
  vsr = __builtin_amdgcn_raw_buffer_load_b128(rs, soff, 0, 0);
  vzr = __builtin_amdgcn_raw_buffer_load_b128(rz, soff, 0, 0);
#pragma unroll
  for (int n = 0; n < 16; ++n) V[n] = __builtin_amdgcn_raw_buffer_load_b128(rv, voff + (n & 7) * 512 + (n >> 3) * 256, 0, AUX_NT);
  __builtin_amdgcn_sched_barrier(0);

  u32x4 qf[4];
  f32x4 c0 = {0.f, 0.f, 0.f, 0.f};
  const bool lo_col = (c & 8) != 0;
#pragma unroll
  for (int s = 0; s < 4; ++s) {
#pragma unroll
    for (int w = 0; w < 4; ++w) {
      const float a0 = bflo(qraw[s][w]) * bflo(ksr[s][w]), a1 = bfhi(qraw[s][w]) * bfhi(ksr[s][w]);
      const unsigned hi = cvtpk(a0, a1);
      const unsigned lo = cvtpk(a0 - bflo(hi), a1 - bfhi(hi));
      qf[s][w] = lo_col ? lo : hi;
    }
    c0 = mfma16(kzr[s] ^ 0x80008000u, qf[s], c0);
  }

  constexpr float C2 = 0.088388347648318440f * 1.4426950408889634f;
  f32x4 acc[8];
#pragma unroll
  for (int i = 0; i < 8; ++i) acc[i] = f32x4{0.f, 0.f, 0.f, 0.f};
  float m = -INFINITY, l = 0.f, corr = 0.f;

  unsigned char* kt = lds + wave * KT_BYTES;
  unsigned char* kw = kt + (lane >> 5) * KPITCH + (lane & 31) * 8;
  const unsigned char* kr = kt + c * KPITCH + g * 16;

  for (int it = 0; it < NIT; ++it) {
    const int nxt = (it + 1) * (32 * 512);
    const int kon = koff + nxt, von = voff + nxt;
#pragma unroll
    for (int n = 0; n < 16; ++n) {
      const i32x4 x = __builtin_bit_cast(i32x4, K[n]);
      u32x2 pk = {cvtpk((float)x[0], (float)x[1]), cvtpk((float)x[2], (float)x[3])};
      const int lam = 16 * ((n >> 1) & 1) + 4 * (n >> 2) + 2 * (n & 1);
      *reinterpret_cast<u32x2*>(kw + lam * KPITCH) = pk;
      K[n] = __builtin_amdgcn_raw_buffer_load_b128(rk, kon + (n & 3) * 1024, (n >> 2) * 4096, AUX_NT);
      if (n & 1) __builtin_amdgcn_sched_barrier(0);
    }
    f32x4 st[2];
#pragma unroll
    for (int t = 0; t < 2; ++t) {
      f32x4 sacc = c0;
#pragma unroll
      for (int s = 0; s < 4; ++s) {
        const u32x4 a = *reinterpret_cast<const u32x4*>(kr + t * 16 * KPITCH + s * 64);
        sacc = mfma16(a, qf[s], sacc);
      }
      st[t] = sacc;
    }
    float sv[8];
#pragma unroll
    for (int j = 0; j < 8; ++j) { const float x = st[j >> 2][j & 3]; sv[j] = (x + dpp<ROR8>(x)) * C2; }
    float mx = fmaxf(fmaxf(fmaxf(sv[0], sv[1]), fmaxf(sv[2], sv[3])), fmaxf(fmaxf(sv[4], sv[5]), fmaxf(sv[6], sv[7])));
    mx = xg_max(mx);
    const float mn = fmaxf(m, mx);
    const float alpha = __builtin_amdgcn_exp2f(m - mn);
    m = mn;
    l *= alpha; corr *= alpha;
    u32x4 pf;
#pragma unroll
    for (int w = 0; w < 4; ++w) {
      const float p0 = __builtin_amdgcn_exp2f(sv[2 * w] - mn), p1 = __builtin_amdgcn_exp2f(sv[2 * w + 1] - mn);
      l += p0 + p1;
      const float a0 = p0 * bflo(vsr[w]), a1 = p1 * bfhi(vsr[w]);
      const unsigned hi = cvtpk(a0, a1);
      const unsigned lo = cvtpk(a0 - bflo(hi), a1 - bfhi(hi));
      const unsigned pp = lo_col ? lo : hi;
      pf[w] = pp;
      corr = fmaf(bflo(pp), bflo(vzr[w]), corr);
      corr = fmaf(bfhi(pp), bfhi(vzr[w]), corr);
    }
    vsr = __builtin_amdgcn_raw_buffer_load_b128(rs, soff, (it + 1) * 64, 0);
    vzr = __builtin_amdgcn_raw_buffer_load_b128(rz, soff, (it + 1) * 64, 0);
#pragma unroll
    for (int i = 0; i < 8; ++i) acc[i] *= alpha;
    __builtin_amdgcn_sched_barrier(0);
#pragma unroll
    for (int h = 0; h < 2; ++h) {
      u32x4 vf[4];
#pragma unroll
      for (int w = 0; w < 4; ++w) {
        const int n0 = h * 8 + 2 * w, n1 = n0 + 1;
        const i32x4 x0 = __builtin_bit_cast(i32x4, V[n0]), x1 = __builtin_bit_cast(i32x4, V[n1]);
#pragma unroll
        for (int e = 0; e < 4; ++e) vf[e][w] = cvtpk((float)x0[e], (float)x1[e]);
        V[n0] = __builtin_amdgcn_raw_buffer_load_b128(rv, von + (n0 & 7) * 512 + (n0 >> 3) * 256, 0, AUX_NT);
        V[n1] = __builtin_amdgcn_raw_buffer_load_b128(rv, von + (n1 & 7) * 512 + (n1 >> 3) * 256, 0, AUX_NT);
        __builtin_amdgcn_sched_barrier(0);
      }
#pragma unroll
      for (int e = 0; e < 4; ++e) acc[h * 4 + e] = mfma16(vf[e], pf, acc[h * 4 + e]);
    }
  }

  l = xg_sum(l);
  corr = xg_sum(corr); corr += dpp<ROR8>(corr);
  float* rec = reinterpret_cast<float*>(lds + wave * KT_BYTES);
  {
    const int hsel = c >> 3;
    float* orow = rec + 16 + (c & 7) * D + 64 * hsel + 16 * g;
#pragma unroll
    for (int r = 0; r < 4; ++r) {
      f32x4 o;
#pragma unroll
      for (int e = 0; e < 4; ++e) {
        const float x0 = acc[e][r] + dpp<ROR8>(acc[e][r]);
        const float x1 = acc[4 + e][r] + dpp<ROR8>(acc[4 + e][r]);
        o[e] = (hsel ? x1 : x0) - corr;
      }
      *reinterpret_cast<f32x4*>(orow + 4 * r) = o;
    }
    if (lane < 8) { rec[lane] = m; rec[8 + lane] = l; }
  }
  __syncthreads();
  {
    float M = -INFINITY;
#pragma unroll
    for (int w = 0; w < WAVES; ++w) M = fmaxf(M, reinterpret_cast<const float*>(lds + w * KT_BYTES)[wave]);
    float L = 0.f, o0 = 0.f, o1 = 0.f;
#pragma unroll
    for (int w = 0; w < WAVES; ++w) {
      const float* r = reinterpret_cast<const float*>(lds + w * KT_BYTES);
      const float wt = __builtin_amdgcn_exp2f(r[wave] - M);
      const f32x2 ov = *reinterpret_cast<const f32x2*>(r + 16 + wave * D + 2 * lane);
      L = fmaf(wt, r[8 + wave], L); o0 = fmaf(wt, ov[0], o0); o1 = fmaf(wt, ov[1], o1);
    }
    float* pr = part + ((size_t)(bh * MROWS + wave) * NSPLIT + split) * (D + 4);
    *reinterpret_cast<f32x2*>(pr + 4 + 2 * lane) = f32x2{o0, o1};
    if (lane == 0) *reinterpret_cast<f32x2*>(pr) = f32x2{M, L};
  }
}

__global__ void __launch_bounds__(256) attn_combine(const float* __restrict__ part, unsigned* __restrict__ out) {
  const int row = blockIdx.x * 4 + (threadIdx.x >> 6), lane = threadIdx.x & 63;
  const float* p0 = part + (size_t)row * NSPLIT * (D + 4);
  float M = -INFINITY;
#pragma unroll
  for (int s = 0; s < NSPLIT; ++s) M = fmaxf(M, p0[s * (D + 4)]);
  float L = 0.f, o0 = 0.f, o1 = 0.f;
#pragma unroll
  for (int s = 0; s < NSPLIT; ++s) {
    const float* p = p0 + s * (D + 4);
    const float wt = __builtin_amdgcn_exp2f(p[0] - M);
    const f32x2 ov = *reinterpret_cast<const f32x2*>(p + 4 + 2 * lane);
    L = fmaf(wt, p[1], L); o0 = fmaf(wt, ov[0], o0); o1 = fmaf(wt, ov[1], o1);
  }
  const float inv = 1.f / L;
  out[(size_t)row * (D / 2) + lane] = cvtpk(o0 * inv, o1 * inv);
}

}

extern "C" void kernel_launch(void* const* d_in, const int* in_sizes, int n_in, void* d_out, int out_size,
                              void* d_ws, size_t ws_size, hipStream_t stream) {
  const unsigned short* q = (const unsigned short*)d_in[0];
  const int* qk = (const int*)d_in[1];
  const int* qv = (const int*)d_in[2];
  const unsigned short* ks = (const unsigned short*)d_in[3];
  const unsigned short* kz = (const unsigned short*)d_in[4];
  const unsigned short* vs = (const unsigned short*)d_in[5];
  const unsigned short* vz = (const unsigned short*)d_in[6];
  float* part = (float*)d_ws;
  attn_split<<<B * HKV * NSPLIT, NTHR, 0, stream>>>(q, qk, qv, ks, kz, vs, vz, part);
  attn_combine<<<B * HKV * MROWS / 4, 256, 0, stream>>>(part, (unsigned*)d_out);
}
```

```cpp
#include <hip/hip_runtime.h>
#include <hip/hip_bf16.h>
#include <cstdint>

namespace {

constexpr int B = 8, HQ = 32, HKV = 16, SQ = 4, SKV = 2048, D = 128, GRP = 128, NGK = SKV / GRP;
constexpr int MROWS = (HQ / HKV) * SQ;
constexpr int WAVES = 4, NTHR = (2 * WAVES + 1) * 64;
constexpr int SLAB = WAVES * 8;
constexpr int NSLAB = SKV / SLAB;
constexpr int SLAB_BYTES = SLAB * D * 4;
constexpr int NP = NSLAB / 2;
static_assert(4 * SLAB == GRP, "a wave's 32-key tile (4 slabs) lies in one K scale group");

#define GAS __attribute__((address_space(1)))
#define LAS __attribute__((address_space(3)))
typedef GAS unsigned gu32;
typedef LAS unsigned lu32;
#define RLX_AGENT __ATOMIC_RELAXED, __HIP_MEMORY_SCOPE_AGENT
#define RLX_WG __ATOMIC_RELAXED, __HIP_MEMORY_SCOPE_WORKGROUP
typedef unsigned u32x4 __attribute__((ext_vector_type(4)));
typedef unsigned u32x2 __attribute__((ext_vector_type(2)));
typedef int i32x4 __attribute__((ext_vector_type(4)));
typedef float f32x4 __attribute__((ext_vector_type(4)));
typedef float f32x2 __attribute__((ext_vector_type(2)));
typedef __bf16 bf16x2 __attribute__((ext_vector_type(2)));
typedef __bf16 bf16x8 __attribute__((ext_vector_type(8)));

__device__ __forceinline__ unsigned cvtpk(float lo, float hi) {
  f32x2 v = {lo, hi};
  return __builtin_bit_cast(unsigned, __builtin_convertvector(v, bf16x2));
}
__device__ __forceinline__ float bflo(unsigned u) { return __builtin_bit_cast(float, u << 16); }
__device__ __forceinline__ float bfhi(unsigned u) { return __builtin_bit_cast(float, u & 0xffff0000u); }
template <int CTRL> __device__ __forceinline__ float dpp(float x) {
  return __builtin_bit_cast(float, __builtin_amdgcn_mov_dpp(__builtin_bit_cast(int, x), CTRL, 0xf, 0xf, true));
}
constexpr int ROR8 = 0x128;
__device__ __forceinline__ float xg_max(float x) {
  auto s = __builtin_amdgcn_permlane16_swap(__float_as_uint(x), __float_as_uint(x), false, false);
  x = fmaxf(__uint_as_float(s[0]), __uint_as_float(s[1]));
  auto t = __builtin_amdgcn_permlane32_swap(__float_as_uint(x), __float_as_uint(x), false, false);
  return fmaxf(__uint_as_float(t[0]), __uint_as_float(t[1]));
}
__device__ __forceinline__ float xg_sum(float x) {
  auto s = __builtin_amdgcn_permlane16_swap(__float_as_uint(x), __float_as_uint(x), false, false);
  x = __uint_as_float(s[0]) + __uint_as_float(s[1]);
  auto t = __builtin_amdgcn_permlane32_swap(__float_as_uint(x), __float_as_uint(x), false, false);
  return __uint_as_float(t[0]) + __uint_as_float(t[1]);
}
__device__ __forceinline__ f32x4 mfma16(u32x4 a, u32x4 b, f32x4 c) {
  return __builtin_amdgcn_mfma_f32_16x16x32_bf16(__builtin_bit_cast(bf16x8, a), __builtin_bit_cast(bf16x8, b), c, 0, 0, 0);
}
template <int N> __device__ __forceinline__ void waitvm() { asm volatile("s_waitcnt vmcnt(%0)" :: "i"(N) : "memory"); }
__device__ __forceinline__ void waitlds() { asm volatile("s_waitcnt lgkmcnt(0)" ::: "memory"); }
__device__ __forceinline__ int rfl(int x) { return __builtin_amdgcn_readfirstlane(x); }
__device__ __forceinline__ unsigned lds_get(lu32* p) { return (unsigned)rfl(__hip_atomic_load(p, RLX_WG)); }
#define DMA_BODY(LD)                                                                                                 \
  "s_mov_b32 %[keep], m0\n\ts_mov_b32 m0, %[m0v]\n\ts_cmp_eq_u32 %[lv], 1\n\ts_cbranch_scc1 1f\n\t"                 \
  "s_cmp_eq_u32 %[lv], 0\n\ts_cbranch_scc1 2f\n\t"                                                                   \
  "s_waitcnt vmcnt(%[n2])\n\t" LD "\n\ts_branch 3f\n"                                                                \
  "1:\n\ts_waitcnt vmcnt(%[n1])\n\t" LD "\n\ts_branch 3f\n"                                                          \
  "2:\n\ts_waitcnt vmcnt(%[n0])\n\t" LD "\n"                                                                         \
  "3:\n\ts_mov_b32 m0, %[keep]"
#define DMA_LD "buffer_load_dwordx4 %[voff], %[rs], %[soff] offen offset:%[off] nt lds"
template <int N0, int N1, int N2, int OFF>
__device__ __forceinline__ void dma_load(int lv, int m0v, int voff, __amdgpu_buffer_rsrc_t r, int soff) {
  int keep;
  asm volatile(DMA_BODY(DMA_LD)
               : [keep] "=&s"(keep)
               : [lv] "s"(lv), [m0v] "s"(m0v), [voff] "v"(voff), [rs] "s"(r), [soff] "s"(soff),
                 [n0] "i"(N0), [n1] "i"(N1), [n2] "i"(N2), [off] "i"(OFF)
               : "scc", "memory");
}
template <int N0, int N1, int N2, int OFF>
__device__ __forceinline__ void dma_load_poll(int lv, int m0v, int voff, __amdgpu_buffer_rsrc_t r, int soff, unsigned ldsaddr,
                                              unsigned& word, unsigned long long& now) {
  int keep;
  asm volatile("ds_read_b32 %[word], %[la]\n\ts_memrealtime %[now]\n\t" DMA_BODY(DMA_LD) "\n\ts_waitcnt lgkmcnt(0)"
               : [keep] "=&s"(keep), [word] "=&v"(word), [now] "=&s"(now)
               : [lv] "s"(lv), [m0v] "s"(m0v), [voff] "v"(voff), [rs] "s"(r), [soff] "s"(soff), [la] "v"(ldsaddr),
                 [n0] "i"(N0), [n1] "i"(N1), [n2] "i"(N2), [off] "i"(OFF)
               : "scc", "memory");
}
__device__ __forceinline__ void lds_put(lu32* p, unsigned v) { __hip_atomic_store(p, v, RLX_WG); }

constexpr int KPITCH = 272;
constexpr int KT_BYTES = 32 * KPITCH;
constexpr int VT_BYTES = NP * 32;
constexpr int KS_BYTES = 2 * (NGK / 2) * D * 2;
constexpr int WV_BYTES = KT_BYTES + VT_BYTES + KS_BYTES;
constexpr int REC_BYTES = MROWS * D * 4 + MROWS * 8;
constexpr int NR = 6;
constexpr int RING_OFF = WAVES * WV_BYTES + 5 * 1024;
constexpr int CTL_OFF = RING_OFF + WAVES * NR * 4096;
constexpr int LDS_BYTES = CTL_OFF + 64;
static_assert(LDS_BYTES <= 160 * 1024 && NR % 2 == 0, "");
static_assert(REC_BYTES <= KT_BYTES && REC_BYTES <= 5 * 1024, "");

constexpr int PAIR_BYTES = 8192, FLAG_OFF = 4608, REQ_OFF = 4864, ACK_OFF = 5120;
constexpr int AUX_SC1 = 16;
constexpr unsigned SPIN_CAP = 1u << 17;

constexpr int DE = 9, DP = 5, DC = 4, TUP = 200, TUC = 240, TA = 30, TB = 30, XP = 4, XC = 14;
static_assert(DE >= DP && DP >= DC && DE <= 4 * (NR - 3) + 1, "ring: lag + 2 units fetched at once must fit");

__global__ void __launch_bounds__(NTHR)
attn_kernel(const unsigned short* __restrict__ q, const int* __restrict__ qk, const int* __restrict__ qv,
            const unsigned short* __restrict__ ks, const unsigned short* __restrict__ kz,
            const unsigned short* __restrict__ vs, const unsigned short* __restrict__ vz,
            unsigned char* ws, unsigned* __restrict__ out) {
  __shared__ __attribute__((aligned(16))) unsigned char lds[LDS_BYTES];
  const int tid = threadIdx.x, lane = tid & 63, wave = rfl(tid >> 6);
  const int g = lane >> 4, c = lane & 15;
  const int bh = blockIdx.x >> 1;
  const bool consumer = ((blockIdx.x ^ (blockIdx.x >> 3)) & 1) != 0;
  const __amdgpu_buffer_rsrc_t rb = __builtin_amdgcn_make_buffer_rsrc((void*)(ws + (size_t)bh * PAIR_BYTES), 0, REC_BYTES, 0x00020000);
  lu32* ctl = (lu32*)(lds + CTL_OFF);

  if (wave == 2 * WAVES) {
    GAS unsigned char* blk = (GAS unsigned char*)ws + (size_t)bh * PAIR_BYTES;
    gu32* pflag = (gu32*)(blk + FLAG_OFF);
    gu32* preq = (gu32*)(blk + REQ_OFF);
    gu32* pack = (gu32*)(blk + ACK_OFF);
    if (lane < 16) ctl[lane] = 0u;
    waitlds();
    __builtin_amdgcn_s_barrier();
    if (!consumer) {
      const unsigned want = (unsigned)rfl(__hip_atomic_load(pack, RLX_AGENT)) + 1u;
      if (lane == 0) __hip_atomic_store(preq, want, RLX_AGENT);
      for (unsigned spins = 0; spins < SPIN_CAP; ++spins) {
        for (int k = 0; k < 24 && lds_get(ctl + 1) == 0u; ++k) __builtin_amdgcn_s_sleep(4);
        if ((unsigned)rfl(__hip_atomic_load(pack, RLX_AGENT)) == want) break;
      }
      while (lds_get(ctl) == 0u) __builtin_amdgcn_s_sleep(8);
      while (lds_get(ctl + 2) != (unsigned)WAVES) __builtin_amdgcn_s_sleep(1);
      if (lane == 0) __hip_atomic_store(pflag, want, RLX_AGENT);
    } else {
      unsigned echoed = 0, pause = 16;
      for (unsigned spins = 0; spins < SPIN_CAP; ++spins) {
        const bool near = lds_get(ctl) != 0u;
        if (near && spins != 0u && (unsigned)rfl(__hip_atomic_load(pflag, RLX_AGENT)) == echoed) break;
        if (!near || spins == 0u || (spins & 7u) == 7u) {
          const unsigned rq = (unsigned)rfl(__hip_atomic_load(preq, RLX_AGENT));
          if (spins == 0u || rq != echoed) {
            if (lane == 0) __hip_atomic_store(pflag, ~rq, RLX_AGENT);
            waitvm<0>();
            if (lane == 0) __hip_atomic_store(pack, rq, RLX_AGENT);
            echoed = rq;
          }
        }
        if (near) {
          if (lds_get(ctl + 1) == 0u) __builtin_amdgcn_s_sleep(4);
        } else {
          for (unsigned k = 0; k < pause && lds_get(ctl) == 0u; ++k) __builtin_amdgcn_s_sleep(4);
          pause *= 2;
        }
      }
      asm volatile("" ::: "memory");
      u32x4 x[5];
#pragma unroll
      for (int k = 0; k < 5; ++k) x[k] = __builtin_amdgcn_raw_buffer_load_b128(rb, k * 1024 + lane * 16, 0, AUX_SC1);
#pragma unroll
      for (int k = 0; k < 5; ++k) *reinterpret_cast<u32x4*>(lds + WAVES * WV_BYTES + k * 1024 + lane * 16) = x[k];
      waitlds();
      if (lane == 0) __hip_atomic_fetch_add(ctl + 1, 1u, RLX_WG);
    }
    return;
  }

  const int s0 = consumer ? NP : 0;
  const int w4 = wave & (WAVES - 1);
  lu32* landed = ctl + 4 + w4;
  lu32* freed = ctl + 8 + w4;
  unsigned char* ring = lds + RING_OFF + w4 * (NR * 4096);

  if (wave >= WAVES) {
    const size_t kvoff = (size_t)bh * SKV * D;
    const __amdgpu_buffer_rsrc_t rk = __builtin_amdgcn_make_buffer_rsrc((void*)(qk + kvoff), 0, SKV * D * 4, 0x00020000);
    const __amdgpu_buffer_rsrc_t rv = __builtin_amdgcn_make_buffer_rsrc((void*)(qv + kvoff), 0, SKV * D * 4, 0x00020000);
    const int voff = w4 * 4096 + lane * 16;
    const int lbase = rfl((int)(size_t)(LAS unsigned char*)ring);
    const int tu = consumer ? TUC : TUP;
    const unsigned t0 = (unsigned)__builtin_amdgcn_s_memrealtime();
    int lvn = 1;
    constexpr int LAG0 = (DE + 2) / 4, LAG1 = (DP + 2) / 4, LAG2 = (DC + 2) / 4;
    int slot = 0;
    unsigned fr = 0;
    __builtin_amdgcn_s_setprio(3);
    for (int n = 0; n < 2 * NP; ++n) {
      const int lv = rfl(n + (consumer ? XC : XP) >= 2 * NP ? 0 : lvn);
      const int lag = lv == 0 ? LAG0 : (lv == 1 ? LAG1 : LAG2);
      const int soff = (s0 + (n >> 3) * 4 + (n & 3)) * SLAB_BYTES;
      const __amdgpu_buffer_rsrc_t rs = (n & 4) ? rv : rk;
      if (n >= NR) {
        while ((int)fr < n - NR + 1) { fr = lds_get(freed); if ((int)fr < n - NR + 1) __builtin_amdgcn_s_sleep(1); }
      }
      const int m0v = lbase + slot * 4096;
      dma_load<DE - 1, DP - 1, DC - 1, 0>(lv, m0v, voff, rs, soff);
      if (n == 2) __builtin_amdgcn_s_barrier();
      if (n > lag && n >= 2) lds_put(landed, (unsigned)(n - lag));
      dma_load<DE - 1, DP - 1, DC - 1, 1024>(lv, m0v, voff, rs, soff);
      dma_load<DE - 1, DP - 1, DC - 1, 2048>(lv, m0v, voff, rs, soff);
      unsigned frv; unsigned long long now;
      dma_load_poll<DE - 1, DP - 1, DC - 1, 3072>(lv, m0v, voff, rs, soff, (unsigned)(size_t)freed, frv, now);
      if (n + 1 >= NR) fr = (unsigned)rfl((int)frv);
      const int late = (int)((unsigned)now - t0) - 100 - ((n * tu) >> 2);
      lvn = late > TA ? 0 : (late < -TB ? 2 : 1);
      slot = slot == NR - 1 ? 0 : slot + 1;
    }
    waitvm<12>(); lds_put(landed, 2 * NP - 3);
    waitvm<8>();  lds_put(landed, 2 * NP - 2);
    waitvm<4>();  lds_put(landed, 2 * NP - 1);
    waitvm<0>();  lds_put(landed, 2 * NP);
    return;
  }

  u32x4 qf[4];
  unsigned char* kt = lds + wave * WV_BYTES;
  unsigned char* kw = kt + (lane >> 5) * KPITCH + (lane & 31) * 8;
  const unsigned char* kr = kt + c * KPITCH + g * 16;
  unsigned char* vt = kt + KT_BYTES;
  const unsigned char* vr = vt + g * 4;
  unsigned char* kst = vt + VT_BYTES;
  const unsigned char* ksl = kst + (lane & 31) * 8;
  {
    u32x4 vst, kss[4];
    const size_t so = ((size_t)bh * NGK + (s0 >> 2)) * D + lane * 8;
    kss[0] = *reinterpret_cast<const u32x4*>(ks + so); kss[1] = *reinterpret_cast<const u32x4*>(ks + so + 512);
    kss[2] = *reinterpret_cast<const u32x4*>(kz + so); kss[3] = *reinterpret_cast<const u32x4*>(kz + so + 512);
    const unsigned short* vp = ((lane & 32) ? vz : vs) + (size_t)bh * SKV + SLAB * (s0 + (lane & 31)) + 8 * wave;
    vst = *reinterpret_cast<const u32x4*>(vp);
    const unsigned short* qp = q + (size_t)bh * (MROWS * D) + (c & 7) * D + 8 * g;
#pragma unroll
    for (int s = 0; s < 4; ++s) qf[s] = *reinterpret_cast<const u32x4*>(qp + 32 * s);
    __builtin_amdgcn_sched_barrier(0);
    __builtin_amdgcn_s_barrier();
    __builtin_amdgcn_sched_barrier(0);
#pragma unroll
    for (int i = 0; i < 4; ++i) *reinterpret_cast<u32x4*>(kst + i * 1024 + lane * 16) = kss[i];
    *reinterpret_cast<u32x4*>(vt + (lane & 31) * 32 + (lane >> 5) * 16) = vst;
  }

  constexpr float SCALE = 0.088388347648318440f;
  constexpr float LOG2E = 1.4426950408889634f;
  f32x4 acc[8];
#pragma unroll
  for (int i = 0; i < 8; ++i) acc[i] = f32x4{0.f, 0.f, 0.f, 0.f};
  float m = -INFINITY, l = 0.f, corr = 0.f;
  const bool lo_col = (c & 8) != 0;
  const unsigned char* rk_ = ring + lane * 16;
  const unsigned char* rv_ = ring + g * 1024 + c * 16;
  unsigned ld = 0;
  int slot = 0;
  u32x4 X[2][4];
  auto fetch2 = [&](int n, bool isv) {
    while ((int)ld <= n + 1) { ld = lds_get(landed); if ((int)ld <= n + 1) __builtin_amdgcn_s_sleep(1); }
    asm volatile("" ::: "memory");
#pragma unroll
    for (int k = 0; k < 2; ++k)
#pragma unroll
      for (int a = 0; a < 4; ++a)
        X[k][a] = *reinterpret_cast<const u32x4*>((isv ? rv_ + (a & 1) * 512 + (a >> 1) * 256 : rk_ + a * 1024) + (slot + k) * 4096);
    asm volatile("" ::: "memory");
    lds_put(freed, (unsigned)(n + 2));
    slot = slot == NR - 2 ? 0 : slot + 2;
    __builtin_amdgcn_sched_barrier(0);
  };

  for (int t = 0; t < NP / 4; ++t) {
    const u32x2 ksr = *reinterpret_cast<const u32x2*>(ksl + t * 256);
    const u32x2 kzr = *reinterpret_cast<const u32x2*>(ksl + t * 256 + KS_BYTES / 2);
    const f32x4 sc = {bflo(ksr[0]), bfhi(ksr[0]), bflo(ksr[1]), bfhi(ksr[1])};
    const f32x4 zc = {bflo(kzr[0]), bfhi(kzr[0]), bflo(kzr[1]), bfhi(kzr[1])};
    u32x4 vf[8], pf;
#pragma unroll
    for (int u = 0; u < 4; ++u) {
      if ((u & 1) == 0) fetch2(8 * t + u, false);
#pragma unroll
      for (int a = 0; a < 4; ++a) {
        const i32x4 x = __builtin_bit_cast(i32x4, X[u & 1][a]);
        const unsigned e0 = cvtpk((float)x[0] - zc[0], (float)x[1] - zc[1]), e1 = cvtpk((float)x[2] - zc[2], (float)x[3] - zc[3]);
        const u32x2 pk = {cvtpk(bflo(e0) * sc[0], bfhi(e0) * sc[1]), cvtpk(bflo(e1) * sc[2], bfhi(e1) * sc[3])};
        *reinterpret_cast<u32x2*>(kw + (16 * (u >> 1) + 2 * (u & 1) + 4 * a) * KPITCH) = pk;
      }
      __builtin_amdgcn_sched_barrier(0);
    }
    {
      f32x4 st[2];
#pragma unroll
      for (int tt = 0; tt < 2; ++tt) {
        f32x4 sacc = {0.f, 0.f, 0.f, 0.f};
#pragma unroll
        for (int s = 0; s < 4; ++s) {
          const u32x4 a = *reinterpret_cast<const u32x4*>(kr + tt * 16 * KPITCH + s * 64);
          sacc = mfma16(a, qf[s], sacc);
        }
        st[tt] = sacc;
      }
      float sv[8];
#pragma unroll
      for (int w = 0; w < 4; ++w) {
        const unsigned r1 = cvtpk(st[w >> 1][2 * (w & 1)], st[w >> 1][2 * (w & 1) + 1]);
        const unsigned r2 = cvtpk(bflo(r1) * SCALE, bfhi(r1) * SCALE);
        sv[2 * w] = bflo(r2) * LOG2E; sv[2 * w + 1] = bfhi(r2) * LOG2E;
      }
      float mx = fmaxf(fmaxf(fmaxf(sv[0], sv[1]), fmaxf(sv[2], sv[3])), fmaxf(fmaxf(sv[4], sv[5]), fmaxf(sv[6], sv[7])));
      mx = xg_max(mx);
      const float mn = fmaxf(m, mx);
      const float alpha = __builtin_amdgcn_exp2f(m - mn);
      m = mn;
      l *= alpha; corr *= alpha;
#pragma unroll
      for (int w = 0; w < 4; ++w) {
        const unsigned vsw = *reinterpret_cast<const unsigned*>(vr + (4 * t + w) * 32);
        const unsigned vzw = *reinterpret_cast<const unsigned*>(vr + (4 * t + w) * 32 + 16);
        const float p0 = __builtin_amdgcn_exp2f(sv[2 * w] - mn), p1 = __builtin_amdgcn_exp2f(sv[2 * w + 1] - mn);
        l += p0 + p1;
        const float a0 = p0 * bflo(vsw), a1 = p1 * bfhi(vsw);
        const unsigned hi = cvtpk(a0, a1);
        const unsigned lo = cvtpk(a0 - bflo(hi), a1 - bfhi(hi));
        const unsigned pp = lo_col ? lo : hi;
        pf[w] = pp;
        corr = fmaf(bflo(pp), bflo(vzw), corr);
        corr = fmaf(bfhi(pp), bfhi(vzw), corr);
      }
#pragma unroll
      for (int i = 0; i < 8; ++i) acc[i] *= alpha;
      __builtin_amdgcn_sched_barrier(0);
    }
    if (t == NP / 4 - 1 && lane == 0) __hip_atomic_fetch_add(ctl, 1u, RLX_WG);
#pragma unroll
    for (int u = 0; u < 4; ++u) {
      if ((u & 1) == 0) fetch2(8 * t + 4 + u, true);
#pragma unroll
      for (int h = 0; h < 2; ++h) {
        const i32x4 x0 = __builtin_bit_cast(i32x4, X[u & 1][2 * h]), x1 = __builtin_bit_cast(i32x4, X[u & 1][2 * h + 1]);
#pragma unroll
        for (int e = 0; e < 4; ++e) {
          unsigned y = cvtpk((float)x0[e], (float)x1[e]);
          asm volatile("" : "+v"(y));
          vf[h * 4 + e][u] = y;
        }
      }
      __builtin_amdgcn_sched_barrier(0);
      if (u == 3) {
#pragma unroll
        for (int i = 0; i < 8; ++i) acc[i] = mfma16(vf[i], pf, acc[i]);
        __builtin_amdgcn_sched_barrier(0);
      }
    }
  }

  const int row = 2 * wave + (lane >> 5), d0 = 4 * (lane & 31);
  unsigned mo = (unsigned)(row * (D * 4) + d0 * 4), mm = (unsigned)(MROWS * D * 4 + row * 8);
  asm volatile("" : "+v"(mo), "+v"(mm));
  l = xg_sum(l);
  corr = xg_sum(corr); corr += dpp<ROR8>(corr);
  {
    float* rec = reinterpret_cast<float*>(kt);
    float* orow = rec + (c & 7) * D + 64 * (c >> 3) + 16 * g;
#pragma unroll
    for (int r = 0; r < 4; ++r) {
      f32x4 o;
#pragma unroll
      for (int e = 0; e < 4; ++e) {
        const float keep = lo_col ? acc[4 + e][r] : acc[e][r], send = lo_col ? acc[e][r] : acc[4 + e][r];
        o[e] = (keep - corr) + dpp<ROR8>(send);
      }
      *reinterpret_cast<f32x4*>(orow + 4 * r) = o;
    }
    if (lane < 8) *reinterpret_cast<f32x2*>(rec + MROWS * D + 2 * lane) = f32x2{m, l};
  }
  waitlds();
  if (lane == 0) __hip_atomic_fetch_add(ctl + 1, 1u, RLX_WG);
  const unsigned nrec = consumer ? WAVES + 1 : WAVES;
  while (lds_get(ctl + 1) != nrec) {}
  asm volatile("" ::: "memory");
  {
    f32x2 ml[WAVES + 1]; f32x4 ow[WAVES + 1];
#pragma unroll
    for (int w = 0; w <= WAVES; ++w) {
      ml[w] = *reinterpret_cast<const f32x2*>(lds + w * WV_BYTES + mm);
      ow[w] = *reinterpret_cast<const f32x4*>(lds + w * WV_BYTES + mo);
    }
    if (!consumer) { ml[WAVES] = f32x2{-INFINITY, 0.f}; ow[WAVES] = f32x4{0.f, 0.f, 0.f, 0.f}; }
    const float M = fmaxf(fmaxf(fmaxf(ml[0][0], ml[1][0]), fmaxf(ml[2][0], ml[3][0])), ml[4][0]);
    float L = 0.f; f32x4 o = {0.f, 0.f, 0.f, 0.f};
#pragma unroll
    for (int w = 0; w <= WAVES; ++w) { const float wt = __builtin_amdgcn_exp2f(ml[w][0] - M); L = fmaf(wt, ml[w][1], L); o += wt * ow[w]; }
    if (!consumer) {
      __builtin_amdgcn_raw_buffer_store_b128(__builtin_bit_cast(u32x4, o), rb, mo, 0, AUX_SC1);
      if ((lane & 31) == 0) __builtin_amdgcn_raw_buffer_store_b64(__builtin_bit_cast(u32x2, f32x2{M, L}), rb, mm, 0, AUX_SC1);
      waitvm<0>();
      if (lane == 0) __hip_atomic_fetch_add(ctl + 2, 1u, RLX_WG);
    } else {
      o *= __builtin_amdgcn_rcpf(L);
      *reinterpret_cast<u32x2*>(reinterpret_cast<unsigned char*>(out + (size_t)bh * (MROWS * D / 2)) + (mo >> 1)) = u32x2{cvtpk(o[0], o[1]), cvtpk(o[2], o[3])};
    }
  }
}

}

extern "C" void kernel_launch(void* const* d_in, const int* in_sizes, int n_in, void* d_out, int out_size,
                              void* d_ws, size_t ws_size, hipStream_t stream) {
  attn_kernel<<<B * HKV * 2, NTHR, 0, stream>>>(
      (const unsigned short*)d_in[0], (const int*)d_in[1], (const int*)d_in[2], (const unsigned short*)d_in[3],
      (const unsigned short*)d_in[4], (const unsigned short*)d_in[5], (const unsigned short*)d_in[6],
      (unsigned char*)d_ws, (unsigned*)d_out);
}
```
